```python
import torch

def _softmax(x, axis=-1):
    return torch.softmax(x, dim=axis)
def _gelu(x):
    return torch.nn.functional.gelu(x, approximate="tanh")
def _silu(x):
    return torch.nn.functional.silu(x)
def _sigmoid(x):
    return torch.sigmoid(x)
def _layernorm(x, axis=-1, eps=1e-5):
    m = x.mean(dim=axis, keepdim=True)
    v = x.var(dim=axis, keepdim=True, unbiased=False)
    return (x - m) / torch.sqrt(v + eps)
def _scatter_add(tgt, idx, src, axis=0):
    return tgt.clone().index_add_(axis, idx, src)
def _segment_sum(src, seg, n):
    out = torch.zeros((n,) + src.shape[1:], dtype=src.dtype)
    return out.index_add_(0, seg, src)
def _pad(x, pw):
    flat = tuple(v for pair in reversed(pw) for v in pair)
    return torch.nn.functional.pad(x, flat)

kernel_name = 'interleave_layers__gqa_block_cached__dense_activation_silu__decode'

def setup_inputs():

    _w = (lambda a: (lambda t: t.to(torch.bfloat16) if t.is_floating_point() else t)(torch.as_tensor(a)))
    def _A_setup():
        B, Sq, D = 8, 1, 512
        Hq, Hkv, Sk, Dh = 32, 32, 4096, 128
        assert Hq % Hkv == 0, (Hq, Hkv)
        return {
            "x": _w((lambda *s: torch.randn(*s, dtype=torch.bfloat16) * 0.1)(B, Sq, D)),
            "k_cache": _w((lambda *s: torch.randn(*s, dtype=torch.bfloat16) * 0.8)(B, Hkv, Sk, Dh)),
            "v_cache": _w((lambda *s: torch.randn(*s, dtype=torch.bfloat16) * 1.6)(B, Hkv, Sk, Dh)),
            "wq": _w((lambda *s: torch.randn(*s, dtype=torch.bfloat16) * 0.8)(D, Hq * Dh) * (D ** -0.5)),
            "wo": _w((lambda *s: torch.randn(*s, dtype=torch.bfloat16) * 1.6)(Hq * Dh, D) * ((Hq * Dh) ** -0.5)),
        }
    _A_in = _A_setup()

    def _B_setup():
        B, S, D, F = 8, 1, 512, 1376
        return {
            "x": _w((lambda *s: torch.randn(*s, dtype=torch.bfloat16) * 0.1)(B, S, D)),
            "w1": _w((lambda *s: torch.randn(*s, dtype=torch.bfloat16) * 0.4)(D, F) * (D ** -0.5)),
            "w2": _w((lambda *s: torch.randn(*s, dtype=torch.bfloat16) * 0.4)(F, D) * (F ** -0.5)),
        }
    _B_in = _B_setup()

    return {
        "A_x": _A_in["x"],
        "A_k_cache": _A_in["k_cache"],
        "A_v_cache": _A_in["v_cache"],
        "A_wq": _A_in["wq"],
        "A_wo": _A_in["wo"],
        "B_x": _B_in["x"],
        "B_w1": _B_in["w1"],
        "B_w2": _B_in["w2"],
    }

def reference(A_x, A_k_cache, A_v_cache, A_wq, A_wo, B_x, B_w1, B_w2):
    def _A_ref(x, k_cache, v_cache, wq, wo):
        B, Sq, D = x.shape
        Hq, Dh = 32, 128
        Hkv = k_cache.shape[1]
        rep = Hq // Hkv
        q = (lambda x, *d: x.permute(*d))((x @ wq).reshape(B, Sq, Hq, Dh), 0, 2, 1, 3)
        k = (lambda x, n, axis: torch.repeat_interleave(x, n, dim=axis))(k_cache, rep, axis=1)
        v = (lambda x, n, axis: torch.repeat_interleave(x, n, dim=axis))(v_cache, rep, axis=1)
        scores = torch.einsum("bhqd,bhkd->bhqk", q, k) * (Dh ** -0.5)
        p = _softmax(scores, axis=-1)
        o = torch.einsum("bhqk,bhkd->bhqd", p, v)
        return (lambda x, *d: x.permute(*d))(o, 0, 2, 1, 3).reshape(B, Sq, Hq * Dh) @ wo

    def _B_ref(x, w1, w2):
        h = torch.einsum("bsd,df->bsf", x, w1)
        h = _silu(h)
        out = torch.einsum("bsf,fd->bsd", h, w2)
        return out

    x = A_x
    x = x + _A_ref(_layernorm(x, axis=-1), A_k_cache, A_v_cache, A_wq, A_wo)
    x = x + _B_ref(_layernorm(x, axis=-1), B_w1, B_w2)
    x = x + _A_ref(_layernorm(x, axis=-1), A_k_cache, A_v_cache, A_wq, A_wo)
    x = x + _B_ref(_layernorm(x, axis=-1), B_w1, B_w2)
    return x
```

```cpp
#include <hip/hip_runtime.h>
#include <hip/hip_bf16.h>
#include <cstdint>

typedef unsigned u32x4 __attribute__((ext_vector_type(4)));
typedef unsigned u32x2 __attribute__((ext_vector_type(2)));
typedef float f32x4 __attribute__((ext_vector_type(4)));
typedef short bf16x8 __attribute__((ext_vector_type(8)));
typedef short bf16x4 __attribute__((ext_vector_type(4)));
typedef unsigned short u16;

constexpr int NB = 8, DM = 512, NH = 32, SK = 4096, DH = 128, FF = 1376;
#define KV_AUX 2

__device__ __forceinline__ float bf2f(u16 b) { return __builtin_bit_cast(float, (unsigned)b << 16); }
__device__ __forceinline__ unsigned cvtpk(float lo, float hi) { unsigned r; asm("v_cvt_pk_bf16_f32 %0, %1, %2" : "=v"(r) : "v"(lo), "v"(hi)); return r; }
__device__ __forceinline__ float rbf(float x) { return __builtin_bit_cast(float, cvtpk(x, x) << 16); }
__device__ __forceinline__ f32x4 mfma32(u32x4 a, u32x4 b, f32x4 c) {
  return __builtin_amdgcn_mfma_f32_16x16x32_bf16(__builtin_bit_cast(bf16x8, a), __builtin_bit_cast(bf16x8, b), c, 0, 0, 0);
}
__device__ __forceinline__ f32x4 mfma16(u32x2 a, u32x2 b, f32x4 c) {
  return __builtin_amdgcn_mfma_f32_16x16x16bf16_1k(__builtin_bit_cast(bf16x4, a), __builtin_bit_cast(bf16x4, b), c, 0, 0, 0);
}
__device__ __forceinline__ float wave_sum(float v) {
  for (int o = 32; o > 0; o >>= 1) v += __shfl_xor(v, o);
  return v;
}
__device__ __forceinline__ float wave_max(float v) {
  for (int o = 32; o > 0; o >>= 1) v = fmaxf(v, __shfl_xor(v, o));
  return v;
}
template <int T> __device__ __forceinline__ unsigned bcast16(unsigned x) {
  return (unsigned)__builtin_amdgcn_update_dpp(0, (int)x, 0x150 + T, 0xf, 0xf, false);
}

__device__ __forceinline__ void ln_row(const float* xin, float* xn, float* red, int tid) {
  float a = xin[tid], b = xin[tid + 256];
  float s = wave_sum(a + b);
  if ((tid & 63) == 0) red[tid >> 6] = s;
  __syncthreads();
  float mean = (red[0] + red[1] + red[2] + red[3]) * (1.f / 512.f);
  __syncthreads();
  float da = a - mean, db = b - mean;
  float v = wave_sum(da * da + db * db);
  if ((tid & 63) == 0) red[tid >> 6] = v;
  __syncthreads();
  float var = (red[0] + red[1] + red[2] + red[3]) * (1.f / 512.f);
  float rs = 1.f / sqrtf(var + 1e-5f);
  xn[tid] = rbf(da * rs); xn[tid + 256] = rbf(db * rs);
  __syncthreads();
}

template <int T> struct TileLoop {
  __device__ static __forceinline__ void pv(const u32x4 (&vt)[4], const unsigned (&pk)[4], unsigned m0, unsigned m1, f32x4& acc0, f32x4& acc1) {
#pragma unroll
    for (int t = 0; t < 4; ++t) {
      unsigned pb = bcast16<T>(pk[t]);
      u32x2 bfrag = {pb & m0, pb & m1};
      acc0 = mfma16(u32x2{vt[t][0], vt[t][1]}, bfrag, acc0);
      acc1 = mfma16(u32x2{vt[t][2], vt[t][3]}, bfrag, acc1);
    }
  }
};

__global__ void __launch_bounds__(256) attn_k(const u16* xbf, const float* xf, const u16* K, const u16* V, const u16* wq, float* o_out) {
  __shared__ float sx[512], sxn[512], red[8], sq[4][128];
  __shared__ __attribute__((aligned(16))) u16 sqb[128];
  __shared__ float sacc[4][128], sm[4], sl[4];
  const int tid = threadIdx.x, lane = tid & 63, w = tid >> 6;
  const int b = blockIdx.x >> 5, h = blockIdx.x & 31;
  if (xbf) { sx[tid] = bf2f(xbf[b * 512 + tid]); sx[tid + 256] = bf2f(xbf[b * 512 + 256 + tid]); }
  else { sx[tid] = xf[b * 512 + tid]; sx[tid + 256] = xf[b * 512 + 256 + tid]; }
  __syncthreads();
  ln_row(sx, sxn, red, tid);
  {
    float a0 = 0, a1 = 0;
    const unsigned* wp = (const unsigned*)(wq + (size_t)(128 * w) * 4096 + h * 128) + lane;
#pragma unroll 8
    for (int k = 0; k < 128; ++k) { unsigned u = wp[(size_t)k * 2048]; float xv = sxn[128 * w + k]; a0 += xv * __builtin_bit_cast(float, u << 16); a1 += xv * __builtin_bit_cast(float, u & 0xffff0000u); }
    sq[w][2 * lane] = a0; sq[w][2 * lane + 1] = a1;
  }
  __syncthreads();
  if (tid < 128) { float q = sq[0][tid] + sq[1][tid] + sq[2][tid] + sq[3][tid]; sqb[tid] = (u16)(cvtpk(q, q) & 0xffff); }
  __syncthreads();
  const int g = lane >> 4, c = lane & 15;
  u32x4 qf[4];
#pragma unroll
  for (int i = 0; i < 4; ++i) qf[i] = *(const u32x4*)(sqb + 32 * i + 8 * g);
  const size_t base = ((size_t)(b * 32 + h) * SK + 1024 * w) * DH * 2;
  auto rk = __builtin_amdgcn_make_buffer_rsrc((void*)((const char*)K + base), 0, 1024 * 256, 0x00020000);
  auto rv = __builtin_amdgcn_make_buffer_rsrc((void*)((const char*)V + base), 0, 1024 * 256, 0x00020000);
  const unsigned koff = c * 256 + g * 16;
  const unsigned voff = g * 1024 + c * 16;
  const unsigned m0 = (c == 0) ? 0xffffu : (c == 1) ? 0xffff0000u : 0u;
  const unsigned m1 = (c == 2) ? 0xffffu : (c == 3) ? 0xffff0000u : 0u;
  const float C2 = 0.08838834764831845f * 1.4426950408889634f;
  float m = -INFINITY, l = 0.f;
  f32x4 acc0 = {0, 0, 0, 0}, acc1 = {0, 0, 0, 0};
  for (int ch = 0; ch < 4; ++ch) {
    f32x4 sall = {0, 0, 0, 0};
#pragma unroll
    for (int T = 0; T < 16; ++T) {
      u32x4 kt[4];
#pragma unroll
      for (int i = 0; i < 4; ++i) kt[i] = __builtin_amdgcn_raw_buffer_load_b128(rk, koff + (ch * 16 + T) * 4096 + i * 64, 0, KV_AUX);
      f32x4 d = {0, 0, 0, 0};
#pragma unroll
      for (int i = 0; i < 4; ++i) d = mfma32(kt[i], qf[i], d);
#pragma unroll
      for (int r = 0; r < 4; ++r) sall[r] = (c == T) ? d[r] : sall[r];
    }
    float mx = fmaxf(fmaxf(sall[0], sall[1]), fmaxf(sall[2], sall[3]));
    mx = wave_max(mx) * C2;
    const float mn = fmaxf(m, mx);
    const float alpha = __builtin_amdgcn_exp2f(m - mn);
    m = mn;
    unsigned pk[4]; float ps = 0;
#pragma unroll
    for (int r = 0; r < 4; ++r) { float p = __builtin_amdgcn_exp2f(sall[r] * C2 - mn); pk[r] = cvtpk(p, p); ps += __builtin_bit_cast(float, pk[r] << 16); }
    l = l * alpha + ps;
    acc0 *= alpha; acc1 *= alpha;
#define PVT(T) { u32x4 vt[4]; _Pragma("unroll") for (int t = 0; t < 4; ++t) vt[t] = __builtin_amdgcn_raw_buffer_load_b128(rv, voff + (ch * 16 + T) * 4096 + t * 256, 0, KV_AUX); TileLoop<T>::pv(vt, pk, m0, m1, acc0, acc1); }
    PVT(0) PVT(1) PVT(2) PVT(3) PVT(4) PVT(5) PVT(6) PVT(7) PVT(8) PVT(9) PVT(10) PVT(11) PVT(12) PVT(13) PVT(14) PVT(15)
  }
  l = wave_sum(l);
  if (c < 4) {
#pragma unroll
    for (int r = 0; r < 4; ++r) { sacc[w][8 * (4 * g + r) + c] = acc0[r]; sacc[w][8 * (4 * g + r) + 4 + c] = acc1[r]; }
  }
  if (lane == 0) { sm[w] = m; sl[w] = l; }
  __syncthreads();
  if (tid < 128) {
    float M = fmaxf(fmaxf(sm[0], sm[1]), fmaxf(sm[2], sm[3]));
    float L = 0, o = 0;
#pragma unroll
    for (int i = 0; i < 4; ++i) { float e = __builtin_amdgcn_exp2f(sm[i] - M); L += e * sl[i]; o += e * sacc[i][tid]; }
    o_out[b * 4096 + h * 128 + tid] = rbf(o / L);
  }
}

template <int K, int N, int MODE>
__global__ void __launch_bounds__(256) gemv_k(const float* in, const u16* W, const u16* xres_bf, const float* xres_f, float* out, u16* out_bf) {
  __shared__ float sin_[8][K];
  __shared__ float tmp[512], red[8];
  __shared__ float part[4][64];
  const int tid = threadIdx.x, lane = tid & 63, w = tid >> 6;
  if (MODE == 1) {
    for (int b = 0; b < 8; ++b) {
      tmp[tid] = in[b * 512 + tid]; tmp[tid + 256] = in[b * 512 + 256 + tid];
      __syncthreads();
      ln_row(tmp, sin_[b], red, tid);
    }
  } else {
    for (int i = tid; i < 8 * K; i += 256) sin_[i / K][i % K] = in[i];
    __syncthreads();
  }
  const int n0 = blockIdx.x * 8;
  float acc[8][8];
#pragma unroll
  for (int b = 0; b < 8; ++b)
#pragma unroll
    for (int j = 0; j < 8; ++j) acc[b][j] = 0;
  for (int k = tid; k < K; k += 256) {
    u32x4 wv = *(const u32x4*)(W + (size_t)k * N + n0);
    float wf[8];
#pragma unroll
    for (int j = 0; j < 4; ++j) { wf[2 * j] = __builtin_bit_cast(float, wv[j] << 16); wf[2 * j + 1] = __builtin_bit_cast(float, wv[j] & 0xffff0000u); }
#pragma unroll
    for (int b = 0; b < 8; ++b) { float xv = sin_[b][k];
#pragma unroll
      for (int j = 0; j < 8; ++j) acc[b][j] += xv * wf[j]; }
  }
#pragma unroll
  for (int b = 0; b < 8; ++b)
#pragma unroll
    for (int j = 0; j < 8; ++j) { float s = wave_sum(acc[b][j]); if (lane == 0) part[w][b * 8 + j] = s; }
  __syncthreads();
  if (tid < 64) {
    const int b = tid >> 3, j = tid & 7;
    float s = rbf(part[0][tid] + part[1][tid] + part[2][tid] + part[3][tid]);
    if (MODE == 1) { out[b * N + n0 + j] = rbf(s / (1.f + __expf(-s))); }
    else {
      float xr = xres_bf ? bf2f(xres_bf[b * N + n0 + j]) : xres_f[b * N + n0 + j];
      float r = rbf(xr + s);
      out[b * N + n0 + j] = r;
      if (MODE == 2) out_bf[b * N + n0 + j] = (u16)(__builtin_bit_cast(unsigned, r) >> 16);
    }
  }
}

extern "C" void kernel_launch(void* const* d_in, const int* in_sizes, int n_in, void* d_out, int out_size, void* d_ws, size_t ws_size, hipStream_t stream) {
  const u16* x = (const u16*)d_in[0]; const u16* K = (const u16*)d_in[1]; const u16* V = (const u16*)d_in[2];
  const u16* wq = (const u16*)d_in[3]; const u16* wo = (const u16*)d_in[4]; const u16* w1 = (const u16*)d_in[6]; const u16* w2 = (const u16*)d_in[7];
  float* ws = (float*)d_ws;
  float* o = ws; float* x1 = ws + 32768; float* hh = x1 + 4096; float* x2 = hh + 8 * 1376; float* x3 = x2 + 4096; float* x4 = x3 + 4096;
  attn_k<<<256, 256, 0, stream>>>(x, nullptr, K, V, wq, o);
  gemv_k<4096, 512, 0><<<64, 256, 0, stream>>>(o, wo, x, nullptr, x1, nullptr);
  gemv_k<512, 1376, 1><<<172, 256, 0, stream>>>(x1, w1, nullptr, nullptr, hh, nullptr);
  gemv_k<1376, 512, 0><<<64, 256, 0, stream>>>(hh, w2, nullptr, x1, x2, nullptr);
  attn_k<<<256, 256, 0, stream>>>(nullptr, x2, K, V, wq, o);
  gemv_k<4096, 512, 0><<<64, 256, 0, stream>>>(o, wo, nullptr, x2, x3, nullptr);
  gemv_k<512, 1376, 1><<<172, 256, 0, stream>>>(x3, w1, nullptr, nullptr, hh, nullptr);
  gemv_k<1376, 512, 2><<<64, 256, 0, stream>>>(hh, w2, nullptr, x3, x4, (u16*)d_out);
}
```

```cpp
#include <hip/hip_runtime.h>
#include <hip/hip_bf16.h>
#include <cstdint>
#include <type_traits>

typedef unsigned u32;
typedef unsigned short u16;
typedef unsigned u32x4 __attribute__((ext_vector_type(4)));
typedef unsigned u32x2 __attribute__((ext_vector_type(2)));
typedef float f32x4 __attribute__((ext_vector_type(4)));
typedef float f32x2 __attribute__((ext_vector_type(2)));
typedef short bf16x8 __attribute__((ext_vector_type(8)));
#define LAS __attribute__((address_space(3)))
#define GAS __attribute__((address_space(1)))

constexpr int NDED = 32, CPH = 7;
#define WINV 7
#define XCV 9
constexpr int NKS = 36, NLDS = 19, XC = XCV, NRK = NKS - NLDS - XC, NREG = NRK + NKS, WIN = WINV, DQ = XC + NLDS;
static_assert(XC <= 9 && WIN <= NRK && WIN <= 9, "");
#define ILV 1
constexpr u32 SSTR = ILV ? 4096u : 1024u, WSTR = ILV ? 1024u : 36u * 1024u;
constexpr int FPC = 44;
constexpr float C2 = 0.08838834764831845f * 1.4426950408889634f;
#define KVAUX 18
constexpr int AUX_NT = 2, AUX_SC1 = 16, AUX_KV = KVAUX;
#define CAUX 0
constexpr int AUX_C = CAUX;
constexpr u32 SPIN_CAP = 1u << 17;

constexpr int WS_CTL = 0;
constexpr int WS_Q1 = 256;
constexpr int WS_Q2 = WS_Q1 + 8 * 32 * 512;
constexpr int P_ROW = 512, P_REC = 1152;
constexpr int WS_P = WS_Q2 + 2 * 32 * 512;
constexpr int WS_Y = WS_P + 2 * 32 * CPH * P_REC;
constexpr int WS_Z = WS_Y + 32 * 32 * 128;
constexpr int WS_X1 = WS_Z + 32 * 32 * 128;
constexpr int WS_X2 = WS_X1 + 8192;
constexpr int WS_END = WS_X2 + 4096;
static_assert(WS_END % 16 == 0, "");

constexpr int SL_SC = 0;
constexpr int SL_CNT = SL_SC + 4 * 2 * 132 * 4;
constexpr int SL_Q = 4352;
constexpr int SL_KV = SL_Q + 2048;
constexpr int SL_END = SL_KV + 4 * 2 * NLDS * 1024;
constexpr int DL_KV = 32768;
constexpr int DL_OWN = 0;
constexpr int DL_Q1 = DL_OWN + 2 * 2 * 4 * 132 * 4;
constexpr int DL_Q2 = DL_Q1 + 8 * 256;
constexpr int DL_O = DL_Q2 + 256;
constexpr int DL_X = DL_O + 512;
constexpr int DL_H = DL_X + 4 * 8 * 256;
constexpr int DL_HB = DL_H + 4 * 2 * 64 * 4;
constexpr int DL_QP = DL_HB + 4 * 2 * 128;
constexpr int DL_XS = DL_QP + 4 * 128 * 4;
constexpr int DL_ST = DL_XS + 8 * 16 * 4;
constexpr int DL_ST2 = DL_ST + 128;
constexpr int DL_FL = DL_ST2 + 64;
constexpr int DL_ZR = DL_FL + 16;
constexpr int DL_END = DL_ZR + 256;
constexpr int LDS_BYTES = 160 * 1024;
static_assert(SL_END + 256 <= LDS_BYTES && DL_END <= DL_KV && DL_KV + 131072 <= LDS_BYTES, "");

struct Args {
  const u16* x; const u16* K; const u16* V; const u16* wq; const u16* wo; const u16* w1; const u16* w2;
  u16* out; unsigned char* ws;
};

__device__ __forceinline__ u32 bmap(u32 n) { return n > 7u ? n : (n >> 1) + 4u * (n & 1u); }
template <int B, int E, class F> __device__ __forceinline__ void sfor(F&& f) {
  if constexpr (B < E) { f(std::integral_constant<int, B>{}); sfor<B + 1, E>(f); }
}
__device__ __forceinline__ float bf2f(u32 b) { return __builtin_bit_cast(float, b << 16); }
typedef __bf16 bf16x2_t __attribute__((ext_vector_type(2)));
__device__ __forceinline__ u32 cvtpk(float lo, float hi) { return __builtin_bit_cast(u32, __builtin_convertvector(f32x2{lo, hi}, bf16x2_t)); }
__device__ __forceinline__ float rbf(float x) { return __builtin_bit_cast(float, cvtpk(x, x) << 16); }
__device__ __forceinline__ f32x4 mfma32(u32x4 a, u32x4 b, f32x4 c) {
  return __builtin_amdgcn_mfma_f32_16x16x32_bf16(__builtin_bit_cast(bf16x8, a), __builtin_bit_cast(bf16x8, b), c, 0, 0, 0);
}
typedef short bf16x4 __attribute__((ext_vector_type(4)));
__device__ __forceinline__ f32x4 mfma16(u32x2 a, u32x2 b, f32x4 c) {
  return __builtin_amdgcn_mfma_f32_16x16x16bf16_1k(__builtin_bit_cast(bf16x4, a), __builtin_bit_cast(bf16x4, b), c, 0, 0, 0);
}
template <int CTRL> __device__ __forceinline__ float dppf(float x) {
  return __builtin_bit_cast(float, __builtin_amdgcn_mov_dpp(__builtin_bit_cast(int, x), CTRL, 0xf, 0xf, true));
}
template <int CTRL> __device__ __forceinline__ u32 dppu(u32 x) { return (u32)__builtin_amdgcn_mov_dpp((int)x, CTRL, 0xf, 0xf, true); }
constexpr int XOR1 = 0xB1, XOR2 = 0x4E, XOR7 = 0x141, XOR15 = 0x140, ROR8 = 0x128, ROR4 = 0x124;
__device__ __forceinline__ float xrow_sum(float x) {
  auto s = __builtin_amdgcn_permlane16_swap(__float_as_uint(x), __float_as_uint(x), false, false);
  x = __uint_as_float(s[0]) + __uint_as_float(s[1]);
  auto t = __builtin_amdgcn_permlane32_swap(__float_as_uint(x), __float_as_uint(x), false, false);
  return __uint_as_float(t[0]) + __uint_as_float(t[1]);
}
__device__ __forceinline__ float xrow_max(float x) {
  auto s = __builtin_amdgcn_permlane16_swap(__float_as_uint(x), __float_as_uint(x), false, false);
  x = fmaxf(__uint_as_float(s[0]), __uint_as_float(s[1]));
  auto t = __builtin_amdgcn_permlane32_swap(__float_as_uint(x), __float_as_uint(x), false, false);
  return fmaxf(__uint_as_float(t[0]), __uint_as_float(t[1]));
}
__device__ __forceinline__ float wave_sum(float x) {
  x += dppf<XOR1>(x); x += dppf<XOR2>(x); x += dppf<ROR4>(x); x += dppf<ROR8>(x);
  return xrow_sum(x);
}
__device__ __forceinline__ float wave_max(float x) {
  x = fmaxf(x, dppf<XOR1>(x)); x = fmaxf(x, dppf<XOR2>(x)); x = fmaxf(x, dppf<ROR4>(x)); x = fmaxf(x, dppf<ROR8>(x));
  return xrow_max(x);
}
#define CBAR() asm volatile("" ::: "memory")
__device__ __forceinline__ u32 opq(u32 x) { asm volatile("" : "+v"(x)); return x; }
__device__ __forceinline__ u32 opqs(u32 x) { asm volatile("" : "+s"(x)); return x; }
#define LGKM0() asm volatile("s_waitcnt lgkmcnt(0)" ::: "memory")
typedef __amdgpu_buffer_rsrc_t rsrc_t;
__device__ __forceinline__ u32x4 ld16_sc1(rsrc_t r, u32 off, u32 so = 0) { return __builtin_amdgcn_raw_buffer_load_b128(r, off, so, AUX_SC1); }
__device__ __forceinline__ void st16_sc1(rsrc_t r, u32 off, u32x4 v, u32 so = 0) { __builtin_amdgcn_raw_buffer_store_b128(v, r, off, so, AUX_SC1); }
__device__ __forceinline__ void st8_sc1(rsrc_t r, u32 off, u32x2 v, u32 so = 0) { __builtin_amdgcn_raw_buffer_store_b64(v, r, off, so, AUX_SC1); }
__device__ __forceinline__ u32x4 make_srd(const void* p, u32 bytes) { return u32x4{(u32)(size_t)p, (u32)((size_t)p >> 32), bytes, 0x00020000u}; }
__device__ __forceinline__ void dma16_nt(u32x4 srd, u32 voff, u32 soff, u32 ldsaddr) {
  u32 keep;
  asm volatile("s_nop 4\n\ts_mov_b32 %0, m0\n\ts_mov_b32 m0, %4\n\ts_nop 0\n\tbuffer_load_dwordx4 %1, %2, %3 offen nt lds\n\ts_mov_b32 m0, %0"
               : "=&s"(keep) : "v"(voff), "s"(srd), "s"(soff), "s"(ldsaddr) : "memory");
}
#define WARM_POL ""
__device__ __forceinline__ void dma4_nt(u32x4 srd, u32 voff, u32 soff, u32 ldsaddr) {
  u32 keep;
  asm volatile("s_nop 4\n\ts_mov_b32 %0, m0\n\ts_mov_b32 m0, %4\n\ts_nop 0\n\tbuffer_load_dword %1, %2, %3 offen" WARM_POL " lds\n\ts_mov_b32 m0, %0"
               : "=&s"(keep) : "v"(voff), "s"(srd), "s"(soff), "s"(ldsaddr) : "memory");
}
__device__ __forceinline__ void dma4_sc1(u32x4 srd, u32 voff, u32 soff, u32 ldsaddr) {
  u32 keep;
  asm volatile("s_nop 4\n\ts_mov_b32 %0, m0\n\ts_mov_b32 m0, %4\n\ts_nop 0\n\tbuffer_load_dword %1, %2, %3 offen sc1 lds\n\ts_mov_b32 m0, %0"
               : "=&s"(keep) : "v"(voff), "s"(srd), "s"(soff), "s"(ldsaddr) : "memory");
}
__device__ __forceinline__ u32 fbits(float f) { return __builtin_bit_cast(u32, f); }
__device__ __forceinline__ float ffrom(u32 u) { return __builtin_bit_cast(float, u); }

__device__ __forceinline__ u32 epoch_load(unsigned char* ws) { return __hip_atomic_load((GAS u32*)(ws + WS_CTL), __ATOMIC_RELAXED, __HIP_MEMORY_SCOPE_AGENT); }
__device__ __forceinline__ u32 epoch_base(u32 ev) { return ((((u32)__builtin_amdgcn_readfirstlane(ev)) >> 4) + 1u) << 4; }
__device__ __forceinline__ bool spin_fail(u32& spins, unsigned char* ws, u32 base) {
  GAS u32* ab = (GAS u32*)(ws + WS_CTL + 128);
  __builtin_amdgcn_s_sleep(1);
  ++spins;
  if (spins > SPIN_CAP) { __hip_atomic_store(ab, base + 15u, __ATOMIC_RELAXED, __HIP_MEMORY_SCOPE_AGENT); return true; }
  return (spins & 63u) == 0u && (u32)__builtin_amdgcn_readfirstlane(__hip_atomic_load(ab, __ATOMIC_RELAXED, __HIP_MEMORY_SCOPE_AGENT)) == base + 15u;
}

struct LaneC { int lane, g, col; bool b0, b1; u32 pm[4]; };
struct QkAcc { float S[3]; float t0, y01; };
template <int I> __device__ __forceinline__ void qk_post(const LaneC& L, f32x4 d, QkAcc& A) {
  const float lo = L.b0 ? d[1] : d[0], hi = L.b0 ? d[3] : d[2];
  const float x = L.b1 ? hi : lo;
  if constexpr ((I & 1) == 0) { A.t0 = x; asm volatile("" : "+v"(A.t0)); }
  else {
    const float y = (L.b0 ? x : A.t0) + dppf<XOR1>(L.b0 ? A.t0 : x);
    if constexpr ((I & 3) == 1) { A.y01 = y; asm volatile("" : "+v"(A.y01)); }
    else {
      const float z = (L.b1 ? y : A.y01) + dppf<XOR2>(L.b1 ? A.y01 : y);
      A.S[I >> 4] = __builtin_bit_cast(float, __builtin_amdgcn_update_dpp(__builtin_bit_cast(int, A.S[I >> 4]), __builtin_bit_cast(int, z), 0xE4, 0xf, 1 << ((I >> 2) & 3), false));
      asm volatile("" : "+v"(A.S[I >> 4]));
    }
  }
}
template <int I> __device__ __forceinline__ void qk_unit(const LaneC& L, u32x4 kf, u32x4 qf, QkAcc& A) {
  qk_post<I>(L, mfma32(kf, qf, f32x4{0.f, 0.f, 0.f, 0.f}), A);
}
template <int I> __device__ __forceinline__ u32x4 pv_prep(const LaneC& L, const u32 (&pk)[3]) {
  const u32 pb = dppu<0x150 + (I & 15)>(pk[I >> 4]);
  return u32x4{pb & L.pm[0], pb & L.pm[1], pb & L.pm[2], pb & L.pm[3]};
}
template <int I> __device__ __forceinline__ void pv_unit(const LaneC& L, u32x4 vf, const u32 (&pk)[3], f32x4& acc) {
  acc = mfma32(vf, pv_prep<I>(L, pk), acc);
}
template <int NS> __device__ __forceinline__ void softmax_unit(const float (&S)[3], u32 (&pk)[3], float& m, float& l) {
  float mx = S[0];
  if constexpr (NS > 1) mx = fmaxf(mx, S[1]);
  if constexpr (NS > 2) mx = fmaxf(mx, S[2]);
  m = wave_max(mx) * C2;
  float ps = 0.f;
#pragma unroll
  for (int r = 0; r < NS; ++r) { const float p = __builtin_amdgcn_exp2f(__builtin_fmaf(S[r], C2, -m)); pk[r] = cvtpk(p, p); ps += bf2f(pk[r] & 0xffffu); }
  l = wave_sum(ps);
}
__device__ __forceinline__ void reset_S(float (&S)[3]) { S[0] = -INFINITY; S[1] = -INFINITY; S[2] = -INFINITY; }
__device__ __forceinline__ void put_partial(const LaneC& L, LAS float* p, f32x4 acc, float m, float l) {
  if (L.col < 8) {
#pragma unroll
    for (int r = 0; r < 4; ++r) p[8 * (4 * L.g + r) + L.col] = acc[r];
  }
  if (L.lane == 0) { p[128] = m; p[129] = l; }
}

struct SState {
  QkAcc A1;
  u32 pk1[3], pk2[3];
  f32x4 acc1, acc2;
  float m1, l1, m2, l2;
  u32x4 q1, q2;
  u32x4 P[NLDS];
  u32x4 bf1;
  u32 nxt;
};

__device__ __forceinline__ void s_publish(const Args& a, const LaneC& L, LAS unsigned char* lds, rsrc_t rw, int w, u32 pbase, u32 base, int rec,
                                          f32x4 accA, float mA, float lA, f32x4 accB, float mB, float lB) {
  volatile LAS u32* cnt = (volatile LAS u32*)(lds + SL_CNT);
  { u32 spins = 0;
#pragma nounroll
    while (cnt[1] < (u32)rec) { if (spin_fail(spins, a.ws, base)) break; } }
  LAS float* sc = (LAS float*)(lds + SL_SC);
  put_partial(L, sc + (w * 2 + 0) * 132, accA, mA, lA);
  put_partial(L, sc + (w * 2 + 1) * 132, accB, mB, lB);
  LGKM0();
  u32 old = 0;
  if (L.lane == 0) old = __hip_atomic_fetch_add((LAS u32*)(lds + SL_CNT), 1u, __ATOMIC_RELAXED, __HIP_MEMORY_SCOPE_WORKGROUP);
  old = __builtin_amdgcn_readfirstlane(old);
  CBAR();
  if (old == (u32)(4 * rec + 3)) {
    const u32 tag = base + rec + 1;
    const u32 rbase = pbase + (rec & 1) * (32 * CPH * P_REC);
    const LAS float* p = sc + (L.lane >> 5) * 132;
    f32x2 ml[4];
#pragma unroll
    for (int i = 0; i < 4; ++i) ml[i] = *(const LAS f32x2*)(p + i * 264 + 128);
    const float M = fmaxf(fmaxf(ml[0][0], ml[1][0]), fmaxf(ml[2][0], ml[3][0]));
    float Ls = 0.f; f32x4 o = {0.f, 0.f, 0.f, 0.f};
#pragma unroll
    for (int i = 0; i < 4; ++i) {
      const float e = __builtin_amdgcn_exp2f(ml[i][0] - M);
      Ls = __builtin_fmaf(e, ml[i][1], Ls);
      o += e * *(const LAS f32x4*)(p + i * 264 + 4 * (L.lane & 31));
    }
    o *= __builtin_amdgcn_rcpf(Ls);
    st16_sc1(rw, L.lane * 16, u32x4{cvtpk(o[0], o[1]), tag, cvtpk(o[2], o[3]), tag}, rbase);
    if ((L.lane & 31) == 0) st16_sc1(rw, L.lane >> 1, u32x4{fbits(M), tag, fbits(Ls), tag}, rbase + 1024);
    LGKM0();
    if (L.lane == 0) cnt[1] = rec + 1;
  }
}

struct SCtx { rsrc_t rk, rv, rw; u32x4 srd; u32 koff, voff, kvbase, qg, pbase, base; const volatile LAS u32x4* ql; int w; };
__device__ __forceinline__ void q_dma(const SCtx& c, u32 so, int slot) {
  if (c.voff < 512u) {
    u32 keep;
    asm volatile("s_nop 4\n\ts_mov_b32 %0, m0\n\ts_mov_b32 m0, %4\n\ts_nop 0\n\tbuffer_load_dwordx4 %1, %2, %3 offen sc1 lds\n\ts_mov_b32 m0, %0"
                 : "=&s"(keep) : "v"(c.voff), "s"(c.srd), "s"(so + c.qg), "s"(SL_Q + slot * 512) : "memory");
  }
}
__device__ __forceinline__ void q_try(const SCtx& c, u32 so, int slot, u32 tag) {
  const u32x4 qa = c.ql[slot * 32 + 1];
  if (!__all(qa[3] == c.base + tag)) q_dma(c, so, slot);
}
__device__ __forceinline__ u32x4 q_read(const SCtx& c, u32 so, int slot, u32 tag, unsigned char* ws, u32* nxt = nullptr) {
  u32 spins = 0;
  tag += c.base;
  u32x4 qa, qb;
#pragma nounroll
  for (;;) {
    qa = c.ql[slot * 32]; qb = c.ql[slot * 32 + 1];
    if (__all(qa[1] == tag && qa[3] == tag && qb[1] == tag && qb[3] == tag)) break;
    if (c.w == 0 || (spins & 3u) == 0u) q_dma(c, so, slot);
    __builtin_amdgcn_s_sleep(2);
    if (spin_fail(spins, ws, c.base)) break;
  }
  if (nxt && spins > 1u) *nxt = (u32)__builtin_amdgcn_s_memrealtime();
  return u32x4{qa[0], qa[2], qb[0], qb[2]};
}

#define SB() __builtin_amdgcn_sched_barrier(0)
#define PACE_S 54
#define PACE_B 60
#define PACE_N 2
#define TAIL_WAIT 600u
template <int D> __device__ __forceinline__ void pace(u32& nxt) {
  asm volatile(
      "1:\n\ts_memrealtime s[96:97]\n\ts_waitcnt lgkmcnt(0)\n\ts_sub_i32 s97, %0, s96\n\ts_cmp_gt_i32 s97, 0\n\ts_cbranch_scc0 2f\n\ts_sleep 1\n\ts_branch 1b\n"
      "2:\n\ts_max_i32 s97, s97, %1\n\ts_add_i32 s96, s96, s97\n\ts_add_i32 %0, s96, %2"
      : "+s"(nxt) : "i"(-PACE_B), "i"(D) : "s96", "s97", "scc", "memory");
}
constexpr int p_of(int id) {
  if (id < XC) return NLDS + NRK + id;
  if (id < DQ) { const int i = id - XC; return i < NRK ? 2 * i + 1 : NRK + i; }
  return 2 * (id - DQ);
}
constexpr int pv2_idx(int id) { return (p_of(id) & ~15) | (4 * (p_of(id) & 3)) | ((p_of(id) >> 2) & 3); }
constexpr int p_kind(int p) { return p < 2 * NRK ? (p & 1) : (p < NRK + NLDS ? 1 : 2); }
constexpr int p_idx(int p) { return p < 2 * NRK ? p >> 1 : (p < NRK + NLDS ? p - NRK : p - NRK - NLDS); }
struct Qk2 { u32x4 qm[4]; f32x4 d; float S[3]; };
__device__ __forceinline__ void qk2_init(const LaneC& L, u32x4 q, Qk2& Q) {
#pragma unroll
  for (int t = 0; t < 4; ++t) { const bool on = (L.col >> 2) == t; Q.qm[t] = u32x4{on ? q[0] : 0u, on ? q[1] : 0u, on ? q[2] : 0u, on ? q[3] : 0u}; }
  reset_S(Q.S);
}
template <int PI> __device__ __forceinline__ void qk2_unit(const LaneC& L, u32x4 kf, Qk2& Q) {
  if constexpr ((PI & 3) == 0) Q.d = mfma32(kf, Q.qm[0], f32x4{0.f, 0.f, 0.f, 0.f}); else Q.d = mfma32(kf, Q.qm[PI & 3], Q.d);
  if constexpr ((PI & 3) == 3) {
    constexpr int T = PI >> 2;
    float e0 = Q.d[0], e1 = Q.d[1], e2 = Q.d[2], e3 = Q.d[3];
    asm volatile("" : "+v"(e0), "+v"(e1), "+v"(e2), "+v"(e3));
    const float lo = L.b0 ? e1 : e0, hi = L.b0 ? e3 : e2;
    float x = L.b1 ? hi : lo;
    x += dppf<XOR1>(x); x += dppf<XOR2>(x);
    Q.S[T >> 2] = ((L.col & 3) == (T & 3)) ? x : Q.S[T >> 2];
    asm volatile("" : "+v"(Q.S[T >> 2]));
  }
}

constexpr int NVP = 9;
__device__ __forceinline__ u32x4 move_home(u32x4 v) {
  u32 a, b, c, d;
  asm volatile("v_accvgpr_write_b32 %0, %1" : "=a"(a) : "v"(v[0]));
  asm volatile("v_accvgpr_write_b32 %0, %1" : "=a"(b) : "v"(v[1]));
  asm volatile("v_accvgpr_write_b32 %0, %1" : "=a"(c) : "v"(v[2]));
  asm volatile("v_accvgpr_write_b32 %0, %1" : "=a"(d) : "v"(v[3]));
  return u32x4{a, b, c, d};
}
static_assert(XC == 9 && NRK == 8 && NLDS == 19, "the tail schedule below is laid out for these sizes");
template <int MODE, int PAR>
__device__ __forceinline__ void s_step(const Args& a, const LaneC& L, LAS unsigned char* lds, const SCtx& c, int n, LAS u32x4* lc, SState& st,
                                       u32x4 (&RC)[NREG], u32x4 (&RP)[NREG]) {
  constexpr int NE = 2 * NKS, VP = MODE == 2 ? NVP : 0;
  const u32 sbase = c.kvbase + bmap(n) * (32u * 4096u * 256u);
  const u32 obase = sbase - (32u * 4096u * 256u);
  const f32x4 Z4 = {0.f, 0.f, 0.f, 0.f};
  u32x4 o0, o1;
  Qk2 Q;
  sfor<0, NE>([&](auto ic) {
    constexpr int e = decltype(ic)::value, ar = e - WIN;
    f32x4 d1 = Z4;
    if constexpr (ar < 0) st.acc1 = mfma32(RP[NREG + ar], st.bf1, st.acc1);
    else if constexpr (ar < XC) d1 = mfma32(st.P[10 + ar], st.q1, Z4);
    else if constexpr (ar < DQ) {
      d1 = mfma32(st.P[ar - XC], st.q1, Z4);
      if constexpr (MODE == 1) lc[(ar - XC) * 64] = st.P[ar - XC];
    } else if constexpr (ar < NKS) d1 = mfma32(RC[ar - DQ], st.q1, Z4);
    else if constexpr (ar < NKS + VP) st.acc1 = mfma32(st.P[9 + ar - NKS], st.bf1, st.acc1);
    else st.acc1 = mfma32(RC[NRK + ar - NKS], st.bf1, st.acc1);
    if constexpr (e < XC) st.P[10 + e] = __builtin_amdgcn_raw_buffer_load_b128(c.rk, c.koff, sbase + e * SSTR, AUX_C);
    else if constexpr (e < DQ) st.P[e - XC] = __builtin_amdgcn_raw_buffer_load_b128(c.rk, c.koff, sbase + e * SSTR, AUX_KV);
    else if constexpr (e < NKS) RC[e - DQ] = __builtin_amdgcn_raw_buffer_load_b128(c.rk, c.koff, sbase + e * SSTR, AUX_KV);
    else if constexpr (e < NKS + VP) st.P[9 + e - NKS] = __builtin_amdgcn_raw_buffer_load_b128(c.rv, c.voff, sbase + (e - NKS) * SSTR, AUX_KV);
    else RC[NRK + e - NKS] = __builtin_amdgcn_raw_buffer_load_b128(c.rv, c.voff, sbase + (e - NKS) * SSTR, AUX_KV);
    SB();
    if constexpr (ar >= 0 && ar < NKS) qk_post<ar>(L, d1, st.A1);
    if constexpr (ar == NKS - 1) { softmax_unit<3>(st.A1.S, st.pk1, st.m1, st.l1); st.acc1 = Z4; }
    if constexpr (ar == -1) {
      s_publish(a, L, lds, c.rw, c.w, c.pbase, c.base, n - 1, st.acc1, st.m1, st.l1, st.acc2, st.m2, st.l2);
      reset_S(st.A1.S);
    }
    if constexpr (MODE == 2) {
      if constexpr (e == DQ - 9) q_try(c, WS_Q2 + PAR * (32 * 512), 2 + PAR, n - 1);
      constexpr int k2 = e - (DQ - 1);
      if constexpr (k2 == 0) { CBAR(); qk2_init(L, q_read(c, WS_Q2 + PAR * (32 * 512), 2 + PAR, n - 1, a.ws, &st.nxt), Q); }
      if constexpr (k2 >= 0 && k2 < NKS / 2) {
        sfor<0, 2>([&](auto i2) {
          constexpr int pi = 2 * k2 + decltype(i2)::value, kd = p_kind(pi), ix = p_idx(pi);
          if constexpr (kd == 0) qk2_unit<pi>(L, RC[ix], Q);
          else if constexpr (kd == 1) {
            qk2_unit<pi>(L, (pi & 1) ? o1 : o0, Q);
            lc[ix * 64] = st.P[ix];
            if constexpr (ix < XC) st.P[ix] = __builtin_amdgcn_raw_buffer_load_b128(c.rk, c.koff, obase + ix * SSTR, AUX_KV);
          } else qk2_unit<pi>(L, st.P[ix], Q);
        });
      }
      if constexpr (k2 >= -1 && k2 + 1 < NKS / 2) {
        if constexpr (p_kind(2 * k2 + 2) == 1) o0 = lc[p_idx(2 * k2 + 2) * 64];
        if constexpr (p_kind(2 * k2 + 3) == 1) o1 = lc[p_idx(2 * k2 + 3) * 64];
      }
      constexpr int f = e + 1 - NKS;
      if constexpr (f == NVP) { softmax_unit<3>(Q.S, st.pk2, st.m2, st.l2); st.acc2 = Z4; }
      if constexpr (f >= NVP && f < NKS) pv_unit<pv2_idx(f)>(L, RC[NRK + f], st.pk2, st.acc2);
      constexpr int bk = f - NVP - 1;
      if constexpr (bk >= 0 && bk < NVP) { pv_unit<pv2_idx(bk)>(L, RC[NRK + bk], st.pk2, st.acc2); RC[NRK + bk] = move_home(st.P[9 + bk]); }
      static_assert(WIN <= 9, "V slot bk must have been consumed on arrival (tick 36 + bk + WIN) before it moves (tail 45 + bk)");
    }
    {
      constexpr int nx = ar + 1;
      if constexpr (nx >= NKS) st.bf1 = pv_prep<nx - NKS>(L, st.pk1);
      else if constexpr (nx < 0) st.bf1 = pv_prep<MODE == 2 ? NKS + nx : pv2_idx(NKS + nx)>(L, st.pk1);
    }
    if constexpr (e % PACE_N == PACE_N - 1) pace<(e & 2) ? PACE_S / 2 : PACE_S - PACE_S / 2>(st.nxt);
    if constexpr (e == NE - 16) q_try(c, WS_Q1 + ((n + 1) & 7) * (32 * 512), 1 - PAR, ((n + 1) & 7) + 1);
    if constexpr (e == NE - 4) st.q1 = q_read(c, WS_Q1 + ((n + 1) & 7) * (32 * 512), 1 - PAR, ((n + 1) & 7) + 1, a.ws);
    SB();
  });
}
static_assert(DQ - 1 + NKS / 2 - 1 == NKS + NVP - 1, "the last unit of the second attention falls in the tail before the first V load to a home register");

#define S0QK 45
constexpr int S0_QK = S0QK, S0_SM = S0_QK + NKS / 3 - 1;
constexpr int s0_done(int e) {
  if (e <= S0_SM) return 0;
  const int x = 2 * (e - S0_SM), y = e - NKS - WIN + 1;
  return x < y ? x : y;
}
static_assert(s0_done(2 * NKS - 1) == NKS - WIN, "");
__device__ __forceinline__ void s_step0(const Args& a, const LaneC& L, SCtx& c, u32 ev, LAS u32x4* lc, SState& st, u32x4 (&RC)[NREG], u32x4 (&RT)[NREG]) {
  const f32x4 Z4 = {0.f, 0.f, 0.f, 0.f};
  Qk2 Q;
  sfor<0, 2 * NKS>([&](auto ic) {
    constexpr int e = decltype(ic)::value, ar = e - WIN;
    if constexpr (ar >= XC && ar < DQ) lc[(ar - XC) * 64] = st.P[ar - XC];
    else if constexpr (ar >= 0) asm volatile("s_waitcnt vmcnt(%0)" :: "i"(WIN - 1) : "memory");
    if constexpr (e < XC) RT[e] = __builtin_amdgcn_raw_buffer_load_b128(c.rk, c.koff, c.kvbase + e * SSTR, AUX_C);
    else if constexpr (e < DQ) st.P[e - XC] = __builtin_amdgcn_raw_buffer_load_b128(c.rk, c.koff, c.kvbase + e * SSTR, AUX_KV);
    else if constexpr (e < NKS) RC[e - DQ] = __builtin_amdgcn_raw_buffer_load_b128(c.rk, c.koff, c.kvbase + e * SSTR, AUX_KV);
    else RC[NRK + e - NKS] = __builtin_amdgcn_raw_buffer_load_b128(c.rv, c.voff, c.kvbase + (e - NKS) * SSTR, AUX_KV);
    SB();
    if constexpr (e == 20) c.base = epoch_base(ev);
    if constexpr (e == S0_QK - 9 || e == S0_QK - 6) q_dma(c, WS_Q1, 0);
    if constexpr (e == S0_QK) { qk2_init(L, q_read(c, WS_Q1, 0, 1u, a.ws, &st.nxt), Q); }
    if constexpr (e >= S0_QK && e <= S0_SM) {
      sfor<0, 3>([&](auto i2) {
        constexpr int pi = 3 * (e - S0_QK) + decltype(i2)::value, kd = p_kind(pi), ix = p_idx(pi);
        if constexpr (kd == 0) qk2_unit<pi>(L, RC[ix], Q);
        else if constexpr (kd == 1) qk2_unit<pi>(L, st.P[ix], Q);
        else qk2_unit<pi>(L, RT[ix], Q);
      });
    }
    if constexpr (e == S0_SM) { softmax_unit<3>(Q.S, st.pk1, st.m1, st.l1); st.acc1 = Z4; }
    sfor<s0_done(e - 1), s0_done(e)>([&](auto i2) { constexpr int i = decltype(i2)::value; pv_unit<pv2_idx(i)>(L, RC[NRK + i], st.pk1, st.acc1); });
    if constexpr (e == 2 * NKS - 22) q_dma(c, WS_Q1 + 32 * 512, 1);
    if constexpr (e == 2 * NKS - 10) st.q1 = q_read(c, WS_Q1 + 32 * 512, 1, 2u, a.ws);
    if constexpr (e == 2 * NKS - 1) st.bf1 = pv_prep<pv2_idx(NKS - WIN)>(L, st.pk1);
    if constexpr (e % PACE_N == PACE_N - 1 && e >= WIN) pace<(e & 2) ? PACE_S / 2 : PACE_S - PACE_S / 2>(st.nxt);
    SB();
  });
}

template <bool PRE>
__device__ __forceinline__ void s_burst(const LaneC& L, const u32x4 (&KL)[NLDS], const LAS u32x4* lc, const u32x4* CP, u32x4 q, u32x4 (&R)[NREG],
                                        f32x4& acc, float& m, float& l) {
  Qk2 Q; u32 pk[3];
  qk2_init(L, q, Q);
  if constexpr (PRE) {
    f32x4 dd[NKS / 4];
    sfor<0, NKS>([&](auto ic) {
      constexpr int pi = decltype(ic)::value, kd = p_kind(pi), ix = p_idx(pi);
      const u32x4 kf = kd == 1 ? KL[kd == 1 ? ix : 0] : kd == 0 ? R[kd == 0 ? ix : 0] : CP[kd == 2 ? ix : 0];
      if constexpr ((pi & 3) == 0) dd[pi >> 2] = mfma32(kf, Q.qm[0], f32x4{0.f, 0.f, 0.f, 0.f}); else dd[pi >> 2] = mfma32(kf, Q.qm[pi & 3], dd[pi >> 2]);
    });
    sfor<0, NKS / 4>([&](auto ic) {
      constexpr int T = decltype(ic)::value;
      float e0 = dd[T][0], e1 = dd[T][1], e2 = dd[T][2], e3 = dd[T][3];
      asm("" : "+v"(e0), "+v"(e1), "+v"(e2), "+v"(e3));
      const float lo = L.b0 ? e1 : e0, hi = L.b0 ? e3 : e2;
      float x = L.b1 ? hi : lo;
      x += dppf<XOR1>(x); x += dppf<XOR2>(x);
      Q.S[T >> 2] = ((L.col & 3) == (T & 3)) ? x : Q.S[T >> 2];
    });
  } else {
    sfor<0, NKS>([&](auto ic) {
      constexpr int pi = decltype(ic)::value, kd = p_kind(pi), ix = p_idx(pi);
      if constexpr (kd == 1) qk2_unit<pi>(L, lc[ix * 64], Q);
      else if constexpr (kd == 0) qk2_unit<pi>(L, R[ix], Q);
      else qk2_unit<pi>(L, CP[ix], Q);
    });
  }
  softmax_unit<3>(Q.S, pk, m, l);
  acc = f32x4{0.f, 0.f, 0.f, 0.f};
  sfor<0, NKS>([&](auto ic) { constexpr int i = decltype(ic)::value; pv_unit<pv2_idx(i)>(L, R[NRK + i], pk, acc); });
}

__device__ __forceinline__ void stream_role(const Args& a, LAS unsigned char* lds, const LaneC& L, int w, int j) {
  const int h = j / CPH, s = j % CPH;
  SCtx c;
  const u32 ev = epoch_load(a.ws);
  c.rk = __builtin_amdgcn_make_buffer_rsrc((void*)a.K, 0, 256u << 20, 0x00020000);
  c.rv = __builtin_amdgcn_make_buffer_rsrc((void*)a.V, 0, 256u << 20, 0x00020000);
  c.rw = __builtin_amdgcn_make_buffer_rsrc((void*)a.ws, 0, WS_END, 0x00020000);
  c.koff = ((L.lane >> 2) & 3) * 256 + L.g * 64 + (L.lane & 3) * 16; c.voff = L.lane * 16;
  c.kvbase = ((u32)h * 4096u + 576u * s) * 256u + w * WSTR;
  c.qg = h * 512;
  c.srd = make_srd(a.ws, WS_END);
  c.ql = (const volatile LAS u32x4*)(lds + SL_Q) + (4 * L.g + (L.col & 3)) * 2;
  c.pbase = WS_P + (h * CPH + s) * P_REC; c.w = w;
  LAS u32x4* lA = (LAS u32x4*)(lds + SL_KV + (w * 2 + 0) * NLDS * 1024) + L.lane;
  LAS u32x4* lB = (LAS u32x4*)(lds + SL_KV + (w * 2 + 1) * NLDS * 1024) + L.lane;
  u32x4 RA[NREG], RB[NREG];
  SState st;
  st.acc2 = f32x4{0, 0, 0, 0}; st.m2 = 0.f; st.l2 = 1.f;
  st.nxt = (u32)__builtin_amdgcn_s_memrealtime();
  auto code_warm = [&]() {
    u32 c0, c1, e0, e1;
    asm volatile("s_getpc_b64 s[96:97]\n\ts_add_u32 %0, s96, _Z7fused_k4Args@rel32@lo+4\n\ts_addc_u32 %1, s97, _Z7fused_k4Args@rel32@hi+12"
                 : "=s"(c0), "=s"(c1) :: "s96", "s97", "scc");
    asm volatile("s_getpc_b64 s[96:97]\n\ts_add_u32 %0, s96, _Z6zero_kPDv4_j@rel32@lo+4\n\ts_addc_u32 %1, s97, _Z6zero_kPDv4_j@rel32@hi+12"
                 : "=s"(e0), "=s"(e1) :: "s96", "s97", "scc");
    const u32 len = e0 - c0;
    const int wv = (j >> 3) * 4 + w;
    if (len < (1u << 18) && (u32)wv * 4096u < len) dma4_nt(u32x4{c0, c1, len, 0x00020000u}, c.voff * 4u, (u32)wv * 4096u, SL_END);
  };
  code_warm();
  {
    const int x = (j + NDED) & 7, wv = (j >> 3) * 4 + w;
    const u32 junk = SL_KV + (w * 2 + 1) * NLDS * 1024;
#pragma unroll
    for (int r = 0; r < 4; ++r) {
      const int t = wv + 112 * r;
      if (t < 128) { const u32 sc = (t & 31) * 64 + L.lane; dma4_nt(make_srd(a.wq, 512 * 4096 * 2), (sc >> 2) * 8192 + (sc & 3) * 64, (x + 8 * (t >> 5)) * 256, junk); }
      else if (t < 256) { const u32 sc = (t & 31) * 64 + L.lane; dma4_nt(make_srd(a.wo, 4096 * 512 * 2), sc * 64, (x + 8 * ((t - 128) >> 5)) * 131072, junk); }
      else if (t < 352) { const u32 sc = ((t - 256) % 24) * 64 + L.lane; dma4_nt(make_srd(a.w1, 512 * 1376 * 2), (sc / 3) * 2752 + min((sc % 3) * 64u, 84u), (x + 8 * ((t - 256) / 24)) * 88, junk); }
      else if (t < 396) { const u32 sc = ((t - 352) % 11) * 64 + L.lane; dma4_nt(make_srd(a.w2, 1376 * 512 * 2), sc * 64, (x + 8 * ((t - 352) / 11)) * 44 * 1024, junk); }
    }
  }
  s_step0(a, L, c, ev, lA, st, RA, RB);
  s_step<1, 1>(a, L, lds, c, 1, lB, st, RB, RA);
  for (int p = 1; p < 4; ++p) {
    s_step<2, 0>(a, L, lds, c, 2 * p, lA, st, RA, RB);
    if (p == 3) code_warm();
    s_step<2, 1>(a, L, lds, c, 2 * p + 1, lB, st, RB, RA);
  }
#pragma unroll
  for (int i = 0; i < XC; ++i) {
    st.P[i] = __builtin_amdgcn_raw_buffer_load_b128(c.rk, c.koff, c.kvbase + bmap(6) * (32u << 20) + i * SSTR, AUX_KV);
    st.P[XC + i] = __builtin_amdgcn_raw_buffer_load_b128(c.rk, c.koff, c.kvbase + bmap(7) * (32u << 20) + i * SSTR, AUX_KV);
  }
  SB();
  sfor<0, WIN>([&](auto ic) { constexpr int i = decltype(ic)::value; pv_unit<NKS - WIN + i>(L, RB[NREG - WIN + i], st.pk1, st.acc1); });
  s_publish(a, L, lds, c.rw, w, c.pbase, c.base, 7, st.acc1, st.m1, st.l1, st.acc2, st.m2, st.l2);
  st.nxt = (u32)__builtin_amdgcn_s_memrealtime() + TAIL_WAIT;
  st.q2 = q_read(c, WS_Q2, 2, 7u, a.ws);
  u32x4 KL[NLDS];
  s_burst<false>(L, KL, lA, st.P, st.q2, RA, st.acc1, st.m1, st.l1);
  SB();
#pragma unroll
  for (int i = 0; i < NLDS; ++i) { KL[i] = lB[i * 64]; asm volatile("" : "+v"(KL[i])); }
  pace<0>(st.nxt);
  st.q2 = q_read(c, WS_Q2 + 32 * 512, 3, 8u, a.ws);
  s_burst<true>(L, KL, lB, st.P + XC, st.q2, RB, st.acc2, st.m2, st.l2);
  s_publish(a, L, lds, c.rw, w, c.pbase, c.base, 8, st.acc1, st.m1, st.l1, st.acc2, st.m2, st.l2);
}

__device__ __forceinline__ u32 perm_lo(u32 a, u32 b) { return __builtin_amdgcn_perm(b, a, 0x05040100u); }
__device__ __forceinline__ u32 perm_hi(u32 a, u32 b) { return __builtin_amdgcn_perm(b, a, 0x07060302u); }
template <bool AG> __device__ __forceinline__ void pin2(u32x4& x, u32x4& y) {
  if constexpr (AG) asm volatile("" : "+a"(x), "+a"(y)); else asm volatile("" : "+v"(x), "+v"(y));
}
template <bool AG> __device__ __forceinline__ void tr8x8(const u32x4* r, u32x4* f) {
#pragma unroll
  for (int c = 0; c < 4; ++c) {
    f[2 * c] = u32x4{perm_lo(r[0][c], r[1][c]), perm_lo(r[2][c], r[3][c]), perm_lo(r[4][c], r[5][c]), perm_lo(r[6][c], r[7][c])};
    f[2 * c + 1] = u32x4{perm_hi(r[0][c], r[1][c]), perm_hi(r[2][c], r[3][c]), perm_hi(r[4][c], r[5][c]), perm_hi(r[6][c], r[7][c])};
    pin2<AG>(f[2 * c], f[2 * c + 1]);
  }
}
__device__ __forceinline__ u32 min3u(u32 a, u32 b, u32 c) { return min(min(a, b), c); }
__device__ __forceinline__ u32 max3u(u32 a, u32 b, u32 c) { return max(max(a, b), c); }
template <int N> __device__ __forceinline__ bool tags_ok(const u32x4 (&v)[N], u32 tag) {
  u32 lo = min(v[0][1], v[0][3]), hi = max(v[0][1], v[0][3]);
#pragma unroll
  for (int i = 1; i < N; ++i) { lo = min3u(lo, v[i][1], v[i][3]); hi = max3u(hi, v[i][1], v[i][3]); }
  return __all(lo == tag && hi == tag);
}
#define LIGHT_AFTER 3u
#define LIGHT_MIN 3
#define DLY_RED 12
#define DLY_GX0 8
#define DLY_GX 36
__device__ __forceinline__ void own_attn(const LaneC& L, LAS unsigned char* lds, int u, int ring, const LAS unsigned char* qb, LAS float* dst) {
  const u32x4 q = *(const LAS u32x4*)(qb + (4 * L.g + (L.col & 3)) * 16);
  const LAS u32x4* t = (const LAS u32x4*)(lds + DL_KV + ring * 32768 + u * 8192) + L.lane;
  QkAcc A; u32 pk[3]; float m, l;
  reset_S(A.S);
  sfor<0, 8>([&](auto ic) { constexpr int i = decltype(ic)::value; qk_unit<i>(L, t[i * 64], q, A); });
  softmax_unit<1>(A.S, pk, m, l);
  f32x4 acc = {0.f, 0.f, 0.f, 0.f};
  sfor<0, 8>([&](auto ic) { constexpr int i = decltype(ic)::value; pv_unit<i>(L, t[(16 + i) * 64], pk, acc); });
  put_partial(L, dst, acc, m, l);
}

__device__ __forceinline__ void ded_role(const Args& a, LAS unsigned char* lds, const LaneC& L, int w, int d) {
  const int lane = L.lane, g = L.g, col = L.col, tid = w * 64 + lane;
  const rsrc_t rk = __builtin_amdgcn_make_buffer_rsrc((void*)a.K, 0, 256u << 20, 0x00020000);
  const rsrc_t rv = __builtin_amdgcn_make_buffer_rsrc((void*)a.V, 0, 256u << 20, 0x00020000);
  const rsrc_t rw = __builtin_amdgcn_make_buffer_rsrc((void*)a.ws, 0, WS_END, 0x00020000);
  const rsrc_t rq = __builtin_amdgcn_make_buffer_rsrc((void*)a.wq, 0, 512 * 4096 * 2, 0x00020000);
  const rsrc_t ro = __builtin_amdgcn_make_buffer_rsrc((void*)a.wo, 0, 4096 * 512 * 2, 0x00020000);
  const rsrc_t r1 = __builtin_amdgcn_make_buffer_rsrc((void*)a.w1, 0, 512 * 1376 * 2, 0x00020000);
  const rsrc_t r2 = __builtin_amdgcn_make_buffer_rsrc((void*)a.w2, 0, 1376 * 512 * 2, 0x00020000);
  const rsrc_t rout = __builtin_amdgcn_make_buffer_rsrc((void*)a.out, 0, 8 * 512 * 2, 0x00020000);
  const u32 ev = epoch_load(a.ws);
  u32 base;
  u32x4 WQ[32], WO[32], W1[16], W2[8];
  u32x2 W2b[8];
  const f32x4 Z4 = {0.f, 0.f, 0.f, 0.f};
  const u32 krow = 64 * (g & 1) + (g >> 1);
  const u32 koff = ((lane >> 2) & 3) * 256 + g * 64 + (lane & 3) * 16, voff = lane * 16;
  const u32 ownbase = ((u32)d * 4096u + 4032u) * 256u;
  const u32x4 sk = make_srd(a.K, 256u << 20), sv = make_srd(a.V, 256u << 20);
  auto own_dma = [&](int b, int i0, int i1) {
    for (int i = i0; i < i1; ++i) {
      dma16_nt(sk, koff, ownbase + bmap(b) * (32u << 20) + i * 1024, DL_KV + (b & 3) * 32768 + i * 1024);
      dma16_nt(sv, voff, ownbase + bmap(b) * (32u << 20) + i * 1024, DL_KV + (b & 3) * 32768 + 16384 + i * 1024);
    }
  };
  float csq0, csq1, cs1;
  {
    u32x4 rawq[32], rawo[32];
    const u32x4 xv0 = *(const GAS u32x4*)((const GAS u16*)a.x + bmap(2 * w) * 512 + lane * 8);
    const u32x4 xv1 = *(const GAS u32x4*)((const GAS u16*)a.x + bmap(2 * w + 1) * 512 + lane * 8);
#define DLY_SETUP 1
    __builtin_amdgcn_s_sleep(DLY_SETUP);
#pragma unroll
    for (int i = 0; i < 32; ++i) rawq[i] = __builtin_amdgcn_raw_buffer_load_b128(rq, krow * 8192 + col * 16, (u32)((128 * w + 8 * (i & 7) + 2 * (i >> 3)) * 8192 + d * 256), AUX_NT);
    __builtin_amdgcn_sched_barrier(0);
#pragma unroll
    for (int i = 0; i < 24; ++i) rawo[i] = __builtin_amdgcn_raw_buffer_load_b128(ro, (u32)(8 * g * 1024 + col * 16), (u32)((128 * d + 32 * (i >> 3) + (i & 7)) * 1024 + w * 256), AUX_NT);
    __builtin_amdgcn_sched_barrier(0);
    base = epoch_base(ev);
#pragma unroll
    for (int rr = 0; rr < 2; ++rr) {
      const int b = 2 * w + rr;
      const u32x4 xv = rr ? xv1 : xv0;
      float v[8];
#pragma unroll
      for (int i = 0; i < 4; ++i) { v[2 * i] = bf2f(xv[i] & 0xffffu); v[2 * i + 1] = ffrom(xv[i] & 0xffff0000u); }
      float sm = 0.f;
#pragma unroll
      for (int i = 0; i < 8; ++i) sm += v[i];
      const float mean = wave_sum(sm) * (1.f / 512.f);
      float sq = 0.f;
#pragma unroll
      for (int i = 0; i < 8; ++i) { v[i] -= mean; sq += v[i] * v[i]; }
      const float rs = 1.f / sqrtf(wave_sum(sq) * (1.f / 512.f) + 1e-5f);
      LAS u16* xp = (LAS u16*)(lds + DL_X + (g * 8 + b) * 256) + col;
#pragma unroll
      for (int i = 0; i < 8; ++i) xp[16 * i] = (u16)(cvtpk(v[i] * rs, 0.f) & 0xffffu);
    }
    if (tid < 128) ((LAS float*)(lds + DL_XS))[tid] = bf2f(((const GAS u16*)a.x)[bmap(tid >> 4) * 512 + 128 * (d >> 3) + 8 * (tid & 15) + (d & 7)]);
#pragma unroll
    for (int ks = 0; ks < 4; ++ks) tr8x8<true>(rawq + 8 * ks, WQ + ks * 8);
    __syncthreads();
    {
      f32x4 D[8];
#pragma unroll
      for (int f = 0; f < 8; ++f) D[f] = Z4;
#pragma unroll
      for (int ks = 0; ks < 4; ++ks) {
        u32x4 af = *(const LAS u32x4*)(lds + DL_X + (w * 8 + (col & 7)) * 256 + ks * 64 + g * 16);
        if (col >= 8) af = u32x4{0x3f803f80u, 0x3f803f80u, 0x3f803f80u, 0x3f803f80u};
#pragma unroll
        for (int f = 0; f < 8; ++f) D[f] = mfma32(af, WQ[ks * 8 + f], D[f]);
      }
      LAS float* qp = (LAS float*)(lds + DL_KV + 3 * 32768);
      if (g < 3) {
#pragma unroll
        for (int r = 0; r < 4; ++r) {
          LAS float* p = qp + (w * 12 + 4 * g + r) * 128 + 8 * col;
          *(LAS f32x4*)p = f32x4{D[0][r], D[1][r], D[2][r], D[3][r]};
          *(LAS f32x4*)(p + 4) = f32x4{D[4][r], D[5][r], D[6][r], D[7][r]};
        }
      }
      __syncthreads();
#pragma unroll
      for (int it = 0; it < 2; ++it) {
        const int b = w + 4 * it;
        f32x2 s = {0.f, 0.f};
#pragma unroll
        for (int i = 0; i < 4; ++i) s += *(const LAS f32x2*)(qp + (i * 12 + b) * 128 + 2 * lane);
        const u32 pr = cvtpk(s[0], s[1]);
        st8_sc1(rw, lane * 8, u32x2{pr, base + (u32)b + 1u}, WS_Q1 + (b * 32 + d) * 512);
        ((LAS u32*)(lds + DL_Q1))[b * 64 + lane] = pr;
      }
      {
        f32x2 s = {0.f, 0.f};
#pragma unroll
        for (int i = 0; i < 4; ++i) s += *(const LAS f32x2*)(qp + (i * 12 + 8) * 128 + 2 * lane);
        csq0 = s[0]; csq1 = s[1];
      }
      __syncthreads();
    }
    own_dma(0, 4 * w, 4 * w + 4);
    {
      const u32x4 sw = make_srd(a.ws, WS_END);
      const u32 junk = DL_KV + 3 * 32768 + w * 256;
      dma4_sc1(sw, lane * 64u, WS_P + (w & 1) * (32 * CPH * P_REC) + d * CPH * P_REC + (w >> 1) * 4096, junk);
      dma4_sc1(sw, lane * 64u, w == 0 ? WS_Y + d * 4096 : w == 1 ? WS_Z + d * 4096 : w == 2 ? WS_X1 : WS_X2, junk);
      if (w == 0) dma4_sc1(sw, (lane & 7) * 64u + (lane >> 3 & 1) * (32 * 512), WS_Q2 + d * 512, junk);
    }
#pragma unroll
    for (int i = 24; i < 32; ++i) rawo[i] = __builtin_amdgcn_raw_buffer_load_b128(ro, (u32)(8 * g * 1024 + col * 16), (u32)((128 * d + 32 * (i >> 3) + (i & 7)) * 1024 + w * 256), AUX_NT);
    __builtin_amdgcn_sched_barrier(0);
#pragma unroll
    for (int ks = 0; ks < 3; ++ks) { tr8x8<false>(rawo + 8 * ks, WO + ks * 8); __builtin_amdgcn_sched_barrier(0); }
    __builtin_amdgcn_sched_barrier(0);
    u32x4 raw2[12];
#pragma unroll
    for (int i = 0; i < 12; ++i) {
      const int u = i < 8 ? 8 * g + i : 32 + 4 * g + (i - 8);
      const bool ok = u < FPC && (FPC * d + u) < 1376;
      const u32 vo = ok ? (u32)((FPC * d + u) * 1024 + w * 256 + col * 16) : 0x7ffffff0u;
      raw2[i] = __builtin_amdgcn_raw_buffer_load_b128(r2, vo, 0, AUX_NT);
    }
    __builtin_amdgcn_sched_barrier(0);
    tr8x8<false>(rawo + 24, WO + 24);
    __builtin_amdgcn_sched_barrier(0);
    u32x2 raw1[32];
    const bool ok1 = col < 11 && (FPC * d + 4 * col) < 1376;
    const u32 vo1 = ok1 ? (u32)(krow * 2752 + (FPC * d + 4 * col) * 2) : 0x7ffffff0u;
#pragma unroll
    for (int i = 0; i < 16; ++i) raw1[i] = __builtin_amdgcn_raw_buffer_load_b64(r1, vo1, (u32)((128 * w + 8 * (i & 7) + 2 * (i >> 3)) * 2752), AUX_NT);
    __builtin_amdgcn_sched_barrier(0);
    tr8x8<true>(raw2, W2);
#pragma unroll
    for (int c = 0; c < 4; ++c) {
      W2b[2 * c] = u32x2{perm_lo(raw2[8][c], raw2[9][c]), perm_lo(raw2[10][c], raw2[11][c])};
      W2b[2 * c + 1] = u32x2{perm_hi(raw2[8][c], raw2[9][c]), perm_hi(raw2[10][c], raw2[11][c])};
      asm volatile("" : "+a"(W2b[2 * c]), "+a"(W2b[2 * c + 1]));
    }
    __builtin_amdgcn_sched_barrier(0);
#pragma unroll
    for (int i = 16; i < 32; ++i) raw1[i] = __builtin_amdgcn_raw_buffer_load_b64(r1, vo1, (u32)((128 * w + 8 * (i & 7) + 2 * (i >> 3)) * 2752), AUX_NT);
    __builtin_amdgcn_sched_barrier(0);
#pragma unroll
    for (int ks = 0; ks < 4; ++ks) {
      const u32x2* r = raw1 + 8 * ks;
#pragma unroll
      for (int c = 0; c < 2; ++c) {
        W1[ks * 4 + 2 * c] = u32x4{perm_lo(r[0][c], r[1][c]), perm_lo(r[2][c], r[3][c]), perm_lo(r[4][c], r[5][c]), perm_lo(r[6][c], r[7][c])};
        W1[ks * 4 + 2 * c + 1] = u32x4{perm_hi(r[0][c], r[1][c]), perm_hi(r[2][c], r[3][c]), perm_hi(r[4][c], r[5][c]), perm_hi(r[6][c], r[7][c])};
        if (ks < 2) pin2<true>(W1[ks * 4 + 2 * c], W1[ks * 4 + 2 * c + 1]); else pin2<false>(W1[ks * 4 + 2 * c], W1[ks * 4 + 2 * c + 1]);
      }
    }
  }
  const int half = lane >> 5, msrc = ((lane & 7) + half) & 7;
  const u32 mloff = (u32)((msrc < CPH ? msrc : CPH - 1) * P_REC + 1024);
  const u32 poff = (u32)(half * P_REC + (lane & 31) * 16), poff3 = (u32)((lane & 31) * 16);
  const u32 roff = (u32)((lane >> 3) * 512 + (lane & 7) * 16);
  const u32 xoff = (u32)(half * 2048 + (lane & 31) * 16);
  LAS float* own = (LAS float*)(lds + DL_OWN);
  {
    const u32x4 ones = {0x3f803f80u, 0x3f803f80u, 0x3f803f80u, 0x3f803f80u};
    f32x4 D[4];
#pragma unroll
    for (int f = 0; f < 4; ++f) { D[f] = Z4;
#pragma unroll
      for (int ks = 0; ks < 4; ++ks) D[f] = mfma32(ones, W1[ks * 4 + f], D[f]); }
    if (g == 0) *(LAS f32x4*)((LAS float*)(lds + DL_H) + w * 128 + 4 * col) = f32x4{D[0][0], D[1][0], D[2][0], D[3][0]};
    asm volatile("s_waitcnt vmcnt(0)" ::: "memory");
    __syncthreads();
    const LAS float* hp = (const LAS float*)(lds + DL_H);
    cs1 = hp[lane] + hp[128 + lane] + hp[256 + lane] + hp[384 + lane];
  }
  own_dma(1, 4 * w, 4 * w + 4);
  if (w >= 2) own_attn(L, lds, w - 2, 0, lds + DL_Q1, own + (w - 2) * 132);
  __syncthreads();
  bool pre = false;
  auto gather_x = [&](u32 vo, u32 so, u32 tag, u32 rowoff, u32x4 (&af)[4], bool first) {
    u32x4 v;
    u32 spins = 0;
    if (tag - base < 8u) { if (first) { if (w == 0) __builtin_amdgcn_s_sleep(DLY_GX0); else if (w == 1 && !pre) __builtin_amdgcn_s_sleep(DLY_GX); } else if (!pre) __builtin_amdgcn_s_sleep(DLY_GX0); }
    else if (first && w == 1) __builtin_amdgcn_s_sleep(DLY_GX / 2);
#pragma nounroll
    for (;;) {
      v = ld16_sc1(rw, vo, so);
      if (__all(v[1] == tag && v[3] == tag)) break;
      if (spin_fail(spins, a.ws, base)) break;
      CBAR();
    }
    *(LAS u32x2*)(lds + DL_X + w * 2048 + half * 256 + (lane & 31) * 8) = u32x2{v[0], v[2]};
    LGKM0();
#pragma unroll
    for (int ks = 0; ks < 4; ++ks) af[ks] = *(const LAS u32x4*)(lds + DL_X + w * 2048 + rowoff + ks * 64 + g * 16);
  };
  auto reduce32 = [&](u32 wsoff, u32 tag, bool dly) -> f32x2 {
    u32x4 v[4];
    u32 spins = 0;
    if (dly) __builtin_amdgcn_s_sleep(DLY_RED);
#pragma nounroll
    for (;;) {
      const u32 ro_ = opq(roff);
#pragma unroll
      for (int u = 0; u < 4; ++u) v[u] = ld16_sc1(rw, ro_ + u * 128, wsoff + d * 4096);
      if (tags_ok(v, tag)) break;
      if (spin_fail(spins, a.ws, base)) break;
      CBAR();
    }
    const float a0 = (bf2f(v[0][0] & 0xffffu) + bf2f(v[1][0] & 0xffffu)) + (bf2f(v[2][0] & 0xffffu) + bf2f(v[3][0] & 0xffffu));
    const float a1 = (ffrom(v[0][0] & 0xffff0000u) + ffrom(v[1][0] & 0xffff0000u)) + (ffrom(v[2][0] & 0xffff0000u) + ffrom(v[3][0] & 0xffff0000u));
    const float c0 = (bf2f(v[0][2] & 0xffffu) + bf2f(v[1][2] & 0xffffu)) + (bf2f(v[2][2] & 0xffffu) + bf2f(v[3][2] & 0xffffu));
    const float c1 = (ffrom(v[0][2] & 0xffff0000u) + ffrom(v[1][2] & 0xffff0000u)) + (ffrom(v[2][2] & 0xffff0000u) + ffrom(v[3][2] & 0xffff0000u));
    auto p = __builtin_amdgcn_permlane32_swap(fbits(a0), fbits(a1), false, false);
    auto q = __builtin_amdgcn_permlane32_swap(fbits(c0), fbits(c1), false, false);
    float x = ffrom(p[0]) + ffrom(p[1]), y = ffrom(q[0]) + ffrom(q[1]);
    x += dppf<ROR8>(x); y += dppf<ROR8>(y);
    auto s2 = __builtin_amdgcn_permlane16_swap(fbits(x), fbits(x), false, false);
    auto t2 = __builtin_amdgcn_permlane16_swap(fbits(y), fbits(y), false, false);
    return f32x2{ffrom(s2[0]) + ffrom(s2[1]), ffrom(t2[0]) + ffrom(t2[1])};
  };
  const u32 oaoff = (u32)(DL_O + (col & 1) * 256 + g * 16);
  const u32 haoff = (u32)(DL_HB + w * 256 + (col & 1) * 128 + g * 16);
  const u32 hboff = (u32)(DL_HB + w * 256 + (col & 1) * 128 + 64 + g * 8);
  ((LAS u32*)(lds + DL_X + w * 2048 + 512))[lane] = 0x3f803f80u;
  const u32 rowoff1 = (col & 3) == 2 ? 512u : (u32)(col & 1) * 256u, rowoff2 = (col & 3) == 2 ? 512u : 0u;

  volatile LAS u32* dflag = (volatile LAS u32*)(lds + DL_FL);
  if (tid == 0) dflag[0] = 0u;
  u32x4 pf[5];
#pragma unroll
  for (int i = 0; i < 5; ++i) pf[i] = u32x4{0u, 0u, 0u, 0u};
  auto p_issue = [&](int kk, int r, u32x4 (&av)[5], u32 dead) {
    const u32 pb = WS_P + (kk & 1) * (32 * CPH * P_REC) + d * CPH * P_REC + dead;
    av[4] = ld16_sc1(rw, mloff, pb + r * 16);
#pragma unroll
    for (int i = 0; i < 3; ++i) av[i] = ld16_sc1(rw, poff, pb + r * P_ROW + 2 * i * P_REC);
    av[3] = ld16_sc1(rw, poff3, pb + r * P_ROW + 6 * P_REC);
  };
  auto stage1 = [&](int kk, int r, u32 try_until, bool usepf) -> bool {
    const u32 tg = base + kk + 1;
    const bool active = (r == 0) || kk >= 2;
    u32 pr = 0u;
    bool good = true;
    if (active) {
      const LAS float* ow = own + ((kk == 8) ? (2 + r) : (r == 0 ? (kk & 1) : 2 + (kk & 1))) * 2 * 132;
      const float mo = fmaxf(ow[128], ow[132 + 128]);
      float lo = 0.f; f32x4 oo = Z4;
#pragma unroll
      for (int i = 0; i < 2; ++i) {
        const float e = __builtin_amdgcn_exp2f(ow[i * 132 + 128] - mo);
        lo = __builtin_fmaf(e, ow[i * 132 + 129], lo);
        oo += e * *(const LAS f32x4*)(ow + i * 132 + 4 * (lane & 31));
      }
      oo *= __builtin_amdgcn_rcpf(lo);
      const u32 pb = WS_P + (kk & 1) * (32 * CPH * P_REC) + d * CPH * P_REC;
      asm volatile("" : "+v"(oo), "+v"(lo));
      u32x4 av[5];
      u32 spins = 0;
      if (usepf) {
#pragma unroll
        for (int i = 0; i < 5; ++i) av[i] = pf[i];
      } else p_issue(kk, r, av, 0u);
#pragma nounroll
      for (;;) {
        if (tags_ok(av, tg)) break;
        bool fail = spin_fail(spins, a.ws, base) || (u32)__builtin_amdgcn_readfirstlane(dflag[0]) == try_until;
#pragma nounroll
        while (!fail && kk < 7 && spins > LIGHT_AFTER && __builtin_popcountll(__ballot(av[4][1] == tg) & 0x7full) < LIGHT_MIN) {
          __builtin_amdgcn_s_sleep(4);
          CBAR();
          av[4] = ld16_sc1(rw, mloff, pb + r * 16);
          fail = spin_fail(spins, a.ws, base) || (u32)__builtin_amdgcn_readfirstlane(dflag[0]) == try_until;
        }
        if (fail) { good = false; break; }
        CBAR();
        p_issue(kk, r, av, 0u);
      }
      const bool isown = msrc == 7;
      const float ms = isown ? mo : ffrom(av[4][0]), ls = isown ? lo : ffrom(av[4][2]);
      float M = fmaxf(ms, dppf<XOR1>(ms)); M = fmaxf(M, dppf<XOR2>(M)); M = fmaxf(M, dppf<XOR7>(M));
      const float el = __builtin_amdgcn_exp2f(ms - M) * ls;
      float Ls = el + dppf<XOR1>(el); Ls += dppf<XOR2>(Ls); Ls += dppf<XOR7>(Ls);
      const float wg = el * __builtin_amdgcn_rcpf(Ls);
      f32x4 o = half ? oo : f32x4{bf2f(av[3][0] & 0xffffu), ffrom(av[3][0] & 0xffff0000u), bf2f(av[3][2] & 0xffffu), ffrom(av[3][2] & 0xffff0000u)};
      o *= dppf<0x156>(wg);
      sfor<0, 3>([&](auto ic) { constexpr int i = decltype(ic)::value;
        o += dppf<0x150 + 2 * i>(wg) * f32x4{bf2f(av[i][0] & 0xffffu), ffrom(av[i][0] & 0xffff0000u), bf2f(av[i][2] & 0xffffu), ffrom(av[i][2] & 0xffff0000u)}; });
      auto t0 = __builtin_amdgcn_permlane32_swap(fbits(o[0]), fbits(o[2]), false, false);
      auto t1 = __builtin_amdgcn_permlane32_swap(fbits(o[1]), fbits(o[3]), false, false);
      pr = cvtpk(ffrom(t0[0]) + ffrom(t0[1]), ffrom(t1[0]) + ffrom(t1[1]));
    }
    ((LAS u32*)(lds + DL_O))[r * 64 + 2 * (lane & 31) + half] = pr;
    return good;
  };
  const u32 yvo = g == 0 ? (u32)(col * 8) : 0x7ffffff0u;
  auto wo_stage = [&](u32 tg) {
    u32x4 af[4];
#pragma unroll
    for (int ks = 0; ks < 4; ++ks) af[ks] = *(const LAS u32x4*)(lds + oaoff + ks * 64);
    const u32 yb = opqs(WS_Y + (8 * w * 32 + d) * 128);
    f32x4 D[8];
#pragma unroll
    for (int f = 0; f < 8; ++f) {
      D[f] = Z4;
#pragma unroll
      for (int ks = 0; ks < 4; ++ks) D[f] = mfma32(af[ks], WO[ks * 8 + f], D[f]);
      if (f >= 1) st8_sc1(rw, yvo, u32x2{cvtpk(D[f - 1][0], D[f - 1][1]), tg}, yb + (f - 1) * 4096);
      SB();
    }
    st8_sc1(rw, yvo, u32x2{cvtpk(D[7][0], D[7][1]), tg}, yb + 7 * 4096);
  };
  for (int k = 0; k <= 8; ++k) {
    const u32 tag = base + k + 1;
    if (k == 8) {
      if (w >= 2) own_attn(L, lds, w - 2, 3, lds + DL_Q2, own + (3 * 2 + w - 2) * 132);
      __syncthreads();
    }
    const int b0 = (k == 8) ? 6 : k, b1 = (k == 8) ? 7 : (k >= 2 ? k - 2 : 0);
    f32x2 xr = *(const LAS f32x2*)((const LAS float*)(lds + DL_XS) + (half ? b1 : b0) * 16 + 2 * (lane & 7));
    if (half && k < 2) xr = f32x2{0.f, 0.f};
    asm volatile("" : "+v"(xr));
    if (!pre) {
      if (w >= 2) stage1(k, w - 2, 16u, true);
      asm volatile("s_waitcnt vmcnt(0)" ::: "memory");
      __syncthreads();
      wo_stage(tag);
    }
    if (w == 1) own_dma(k + 2, 0, 16);
    if (w >= 2 && k >= 1 && k <= 7) own_attn(L, lds, w - 2, (k - 1) & 3, lds + DL_Q2, own + ((2 + ((k - 1) & 1)) * 2 + w - 2) * 132);
    if (w >= 2 && k <= 6) own_attn(L, lds, w - 2, (k + 1) & 3, lds + DL_Q1 + (k + 1) * 256, own + (((k + 1) & 1) * 2 + w - 2) * 132);
    if (w == 0) {
      const f32x2 y = reduce32(WS_Y, tag, !pre && k < 7);
      const u32 pr = cvtpk(xr[0] + rbf(y[0]), xr[1] + rbf(y[1]));
      if ((lane & 24) == 0) st8_sc1(rw, (u32)(half * 2048 + (lane & 7) * 8), u32x2{pr, tag}, WS_X1 + d * 64);
      xr = f32x2{bf2f(pr & 0xffffu), ffrom(pr & 0xffff0000u)};
    }
    {
      u32x4 af[4];
      gather_x(xoff, WS_X1 + w * 512, tag, rowoff1, af, true);
      f32x4 D[4], Ds = Z4;
#pragma unroll
      for (int f = 0; f < 4; ++f) { D[f] = Z4;
#pragma unroll
        for (int ks = 0; ks < 4; ++ks) D[f] = mfma32(af[ks], W1[ks * 4 + f], D[f]); }
#pragma unroll
      for (int ks = 0; ks < 4; ++ks) Ds = mfma32(af[ks], af[ks], Ds);
      if (g == 0) {
        LAS float* hp = (LAS float*)(lds + DL_H) + w * 128 + 4 * col;
        *(LAS f32x4*)hp = f32x4{D[0][0], D[1][0], D[2][0], D[3][0]};
        *(LAS f32x4*)(hp + 64) = f32x4{D[0][1], D[1][1], D[2][1], D[3][1]};
        if (col < 4) *(LAS f32x2*)((LAS float*)(lds + DL_ST) + w * 8 + 2 * col) = f32x2{Ds[0], Ds[1]};
      }
    }
    __syncthreads();
    {
      const LAS float* hp = (const LAS float*)(lds + DL_H);
      const LAS float* sp = (const LAS float*)(lds + DL_ST);
      float ha[4], hb_[4]; f32x4 sa[4]; f32x2 sb[4];
#pragma unroll
      for (int i = 0; i < 4; ++i) { sa[i] = *(const LAS f32x4*)(sp + i * 8); sb[i] = *(const LAS f32x2*)(sp + i * 8 + 4); }
#pragma unroll
      for (int i = 0; i < 4; ++i) { ha[i] = hp[i * 128 + lane]; hb_[i] = hp[i * 128 + 64 + lane]; }
      SB();
      float h0 = (ha[0] + ha[1]) + (ha[2] + ha[3]), h1 = (hb_[0] + hb_[1]) + (hb_[2] + hb_[3]);
      const float q0 = (sa[0][0] + sa[1][0]) + (sa[2][0] + sa[3][0]), q1 = (sa[0][3] + sa[1][3]) + (sa[2][3] + sa[3][3]);
      const float s0 = (sb[0][0] + sb[1][0]) + (sb[2][0] + sb[3][0]), s1 = (sb[0][1] + sb[1][1]) + (sb[2][1] + sb[3][1]);
      const float mean0 = s0 * (1.f / 512.f), mean1 = s1 * (1.f / 512.f);
      const float rs0 = __builtin_amdgcn_rsqf(fmaxf(__builtin_fmaf(-mean0, mean0, q0 * (1.f / 512.f)), 0.f) + 1e-5f);
      const float rs1 = __builtin_amdgcn_rsqf(fmaxf(__builtin_fmaf(-mean1, mean1, q1 * (1.f / 512.f)), 0.f) + 1e-5f);
      h0 = rbf(rs0 * __builtin_fmaf(-mean0, cs1, h0)); h1 = rbf(rs1 * __builtin_fmaf(-mean1, cs1, h1));
      h0 = h0 * __builtin_amdgcn_rcpf(1.f + __builtin_amdgcn_exp2f(-1.4426950408889634f * h0));
      h1 = h1 * __builtin_amdgcn_rcpf(1.f + __builtin_amdgcn_exp2f(-1.4426950408889634f * h1));
      const u32 pr = cvtpk(h0, h1);
      LAS u16* hb = (LAS u16*)(lds + DL_HB + w * 256);
      hb[lane] = (u16)(pr & 0xffffu); hb[64 + lane] = (u16)(pr >> 16);
      LGKM0();
      const u32x4 af = *(const LAS u32x4*)(lds + haoff);
      const u32x2 ab = *(const LAS u32x2*)(lds + hboff);
      const u32 yb = opqs(WS_Z + (8 * w * 32 + d) * 128);
      f32x4 D[8];
#pragma unroll
      for (int f = 0; f < 8; ++f) D[f] = mfma32(af, W2[f], Z4);
      SB();
#pragma unroll
      for (int f = 0; f < 8; ++f) {
        D[f] = mfma16(ab, W2b[f], D[f]);
        if (f >= 3) st8_sc1(rw, yvo, u32x2{cvtpk(D[f - 3][0], D[f - 3][1]), tag}, yb + (f - 3) * 4096);
        SB();
      }
#pragma unroll
      for (int f = 5; f < 8; ++f) st8_sc1(rw, yvo, u32x2{cvtpk(D[f][0], D[f][1]), tag}, yb + f * 4096);
    }
    if (w >= 2 && k <= 6) { const bool ok = stage1(k + 1, w - 2, tag, false); if (lane == 0) dflag[w - 1] = ok ? tag : 0u; }
    if (w == 0) {
      const f32x2 z = reduce32(WS_Z, tag, k < 7);
      const u32 pr = cvtpk(xr[0] + rbf(z[0]), xr[1] + rbf(z[1]));
      if (k <= 7 && lane < 8) {
        st8_sc1(rw, (u32)(lane * 8), u32x2{pr, tag}, WS_X2 + d * 64);
        *(LAS f32x2*)((LAS float*)(lds + DL_XS) + b0 * 16 + 2 * lane) = f32x2{bf2f(pr & 0xffffu), ffrom(pr & 0xffff0000u)};
      }
      if ((lane & 24) == 0 && (half ? k >= 2 : k == 8)) {
        const u32 oo = (u32)(bmap(half ? b1 : b0) * 1024 + (lane & 7) * 32);
        __builtin_amdgcn_raw_buffer_store_b16((u16)(pr & 0xffffu), rout, oo, 256 * (d >> 3) + 2 * (d & 7), 0);
        __builtin_amdgcn_raw_buffer_store_b16((u16)(pr >> 16), rout, oo + 16, 256 * (d >> 3) + 2 * (d & 7), 0);
      }
    }
    if (k == 8) {
      if (d == 0 && w == 0 && lane == 0) __hip_atomic_store((GAS u32*)(a.ws + WS_CTL), base, __ATOMIC_RELAXED, __HIP_MEMORY_SCOPE_AGENT);
      break;
    }
    if (w == 0 && lane == 0) dflag[0] = tag;
    __syncthreads();
    pre = __builtin_amdgcn_readfirstlane((k <= 6 && dflag[1] == tag && dflag[2] == tag) ? 1 : 0) != 0;
    if (pre) wo_stage(tag + 1);
    {
      u32x4 af[4];
      gather_x(poff3, WS_X2 + w * 512, tag, rowoff2, af, false);
      p_issue(k + 1, w & 1, pf, (w >= 2 && !pre && k < 7) ? 0u : 0x40000000u);
      f32x4 D[8], Ds = Z4;
#pragma unroll
      for (int ks = 0; ks < 4; ++ks) Ds = mfma32(af[ks], af[ks], Ds);
#pragma unroll
      for (int f = 0; f < 8; ++f) { D[f] = Z4;
#pragma unroll
        for (int ks = 0; ks < 4; ++ks) D[f] = mfma32(af[ks], WQ[ks * 8 + f], D[f]); }
      if (g == 0) {
        LAS float* p = (LAS float*)(lds + DL_QP) + w * 128 + 8 * col;
        *(LAS f32x4*)p = f32x4{D[0][0], D[1][0], D[2][0], D[3][0]};
        *(LAS f32x4*)(p + 4) = f32x4{D[4][0], D[5][0], D[6][0], D[7][0]};
        if (col < 4) ((LAS float*)(lds + DL_ST2))[w * 4 + col] = Ds[0];
      }
    }
    __syncthreads();
    if (w == 0) {
      f32x2 qp_[4]; f32x4 sa[4];
#pragma unroll
      for (int i = 0; i < 4; ++i) { sa[i] = *(const LAS f32x4*)((const LAS float*)(lds + DL_ST2) + i * 4); qp_[i] = *(const LAS f32x2*)((const LAS float*)(lds + DL_QP) + i * 128 + 2 * lane); }
      SB();
      const f32x2 s = (qp_[0] + qp_[1]) + (qp_[2] + qp_[3]);
      const float q0 = (sa[0][0] + sa[1][0]) + (sa[2][0] + sa[3][0]), s0 = (sa[0][2] + sa[1][2]) + (sa[2][2] + sa[3][2]);
      const float mean = s0 * (1.f / 512.f);
      const float rs = __builtin_amdgcn_rsqf(fmaxf(__builtin_fmaf(-mean, mean, q0 * (1.f / 512.f)), 0.f) + 1e-5f);
      const u32 pr = cvtpk(rs * __builtin_fmaf(-mean, csq0, s[0]), rs * __builtin_fmaf(-mean, csq1, s[1]));
      st8_sc1(rw, lane * 8, u32x2{pr, tag}, WS_Q2 + ((k & 1) * 32 + d) * 512);
      ((LAS u32*)(lds + DL_Q2))[lane] = pr;
    }
    __syncthreads();
  }
}

__device__ __forceinline__ LaneC make_lane() {
  LaneC L;
  L.lane = opq(threadIdx.x) & 63; L.g = L.lane >> 4; L.col = L.lane & 15; L.b0 = (L.lane & 1) != 0; L.b1 = (L.lane & 2) != 0;
#pragma unroll
  for (int i = 0; i < 4; ++i) L.pm[i] = (L.col == 2 * i) ? 0xffffu : (L.col == 2 * i + 1) ? 0xffff0000u : 0u;
  return L;
}
__global__ void __launch_bounds__(256, 1) fused_k(Args a) {
  extern __shared__ __attribute__((aligned(16))) unsigned char smem[];
  LAS unsigned char* lds = (LAS unsigned char*)smem;
  const int w = __builtin_amdgcn_readfirstlane(threadIdx.x >> 6);
  if (blockIdx.x < NDED) {
    const LaneC L = make_lane();
    ded_role(a, lds, L, w, blockIdx.x);
  } else {
    if (threadIdx.x < 4) ((LAS u32*)(lds + SL_CNT))[threadIdx.x] = 0u;
    ((LAS u32x2*)(lds + SL_Q))[threadIdx.x] = u32x2{0u, 0u};
    __syncthreads();
    const LaneC L = make_lane();
    stream_role(a, lds, L, w, blockIdx.x - NDED);
  }
}

__global__ void zero_k(u32x4* p) { p[blockIdx.x * 256 + threadIdx.x] = u32x4{0u, 0u, 0u, 0u}; }
extern "C" void kernel_launch(void* const* d_in, const int* in_sizes, int n_in, void* d_out, int out_size, void* d_ws, size_t ws_size, hipStream_t stream) {
  Args a;
  a.x = (const u16*)d_in[0]; a.K = (const u16*)d_in[1]; a.V = (const u16*)d_in[2]; a.wq = (const u16*)d_in[3]; a.wo = (const u16*)d_in[4];
  a.w1 = (const u16*)d_in[6]; a.w2 = (const u16*)d_in[7]; a.out = (u16*)d_out; a.ws = (unsigned char*)d_ws;
  (void)hipFuncSetAttribute((const void*)fused_k, hipFuncAttributeMaxDynamicSharedMemorySize, LDS_BYTES);
  hipLaunchKernelGGL(fused_k, dim3(256), dim3(256), LDS_BYTES, stream, a);
}
```
